```python
import torch

def _softmax(x, axis=-1):
    return torch.softmax(x, dim=axis)
def _gelu(x):
    return torch.nn.functional.gelu(x, approximate="tanh")
def _silu(x):
    return torch.nn.functional.silu(x)
def _sigmoid(x):
    return torch.sigmoid(x)
def _layernorm(x, axis=-1, eps=1e-5):
    m = x.mean(dim=axis, keepdim=True)
    v = x.var(dim=axis, keepdim=True, unbiased=False)
    return (x - m) / torch.sqrt(v + eps)
def _scatter_add(tgt, idx, src, axis=0):
    return tgt.clone().index_add_(axis, idx, src)
def _segment_sum(src, seg, n):
    out = torch.zeros((n,) + src.shape[1:], dtype=src.dtype)
    return out.index_add_(0, seg, src)
def _pad(x, pw):
    flat = tuple(v for pair in reversed(pw) for v in pair)
    return torch.nn.functional.pad(x, flat)

kernel_name = 'interleave_layers__grouped_kv_mqa__glu_gated_geglu__prompt'

def setup_inputs():

    _w = (lambda a: (lambda t: t.to(torch.bfloat16) if t.is_floating_point() else t)(torch.as_tensor(a)))
    def _A_setup():
        B, S, D, H = 1, 2048, 640, 32
        Dh = D // H
        return {
            "x": _w((lambda *s: torch.randn(*s, dtype=torch.bfloat16) * 0.05)(B, S, D)),
            "wq": _w((lambda *s: torch.randn(*s, dtype=torch.bfloat16) * 0.05)(D, H * Dh)),
            "wk": _w((lambda *s: torch.randn(*s, dtype=torch.bfloat16) * 0.05)(D, Dh)),
            "wv": _w((lambda *s: torch.randn(*s, dtype=torch.bfloat16) * 0.05)(D, Dh)),
            "wo": _w((lambda *s: torch.randn(*s, dtype=torch.bfloat16) * 0.05)(H * Dh, D)),
        }
    _A_in = _A_setup()

    def _B_setup():
        B, S, Dm, Df = 1, 2048, 640, 1728
        return {
            "x": _w((lambda *s: torch.randn(*s, dtype=torch.bfloat16) * 0.05)(B, S, Dm)),
            "w_gate": _w((lambda *s: torch.randn(*s, dtype=torch.bfloat16) * 0.8)(Dm, Df) * (Dm ** -0.5)),
            "w_up": _w((lambda *s: torch.randn(*s, dtype=torch.bfloat16) * 0.8)(Dm, Df) * (Dm ** -0.5)),
            "w_down": _w((lambda *s: torch.randn(*s, dtype=torch.bfloat16) * 0.8)(Df, Dm) * (Df ** -0.5)),
        }
    _B_in = _B_setup()

    return {
        "A_x": _A_in["x"],
        "A_wq": _A_in["wq"],
        "A_wk": _A_in["wk"],
        "A_wv": _A_in["wv"],
        "A_wo": _A_in["wo"],
        "B_x": _B_in["x"],
        "B_w_gate": _B_in["w_gate"],
        "B_w_up": _B_in["w_up"],
        "B_w_down": _B_in["w_down"],
    }

def reference(A_x, A_wq, A_wk, A_wv, A_wo, B_x, B_w_gate, B_w_up, B_w_down):
    def _A_ref(x, wq, wk, wv, wo):
        B, S, D = x.shape
        H = 32
        Dh = D // H
        q = (lambda x, *d: x.permute(*d))((x @ wq).reshape(B, S, H, Dh), 0, 2, 1, 3)
        k = x @ wk
        v = x @ wv
        scores = torch.einsum("bhqd,bkd->bhqk", q, k) * (Dh ** -0.5)
        p = _softmax(scores, axis=-1)
        o = torch.einsum("bhqk,bkd->bhqd", p, v)
        return (lambda x, *d: x.permute(*d))(o, 0, 2, 1, 3).reshape(B, S, H * Dh) @ wo

    def _B_ref(x, w_gate, w_up, w_down):
        g = torch.einsum("bsd,df->bsf", x, w_gate)
        u = torch.einsum("bsd,df->bsf", x, w_up)
        h = _gelu(g) * u
        return torch.einsum("bsf,fd->bsd", h, w_down)

    x = A_x
    x = x + _A_ref(_layernorm(x, axis=-1), A_wq, A_wk, A_wv, A_wo)
    x = x + _B_ref(_layernorm(x, axis=-1), B_w_gate, B_w_up, B_w_down)
    x = x + _A_ref(_layernorm(x, axis=-1), A_wq, A_wk, A_wv, A_wo)
    x = x + _B_ref(_layernorm(x, axis=-1), B_w_gate, B_w_up, B_w_down)
    return x
```

```cpp
#include <hip/hip_runtime.h>
#include <hip/hip_bf16.h>
#include <stdint.h>

typedef unsigned short u16;
typedef __attribute__((ext_vector_type(8))) short bf16x8;
typedef __attribute__((ext_vector_type(4))) short bf16x4;
typedef __attribute__((ext_vector_type(2))) __bf16 bf2_t;
typedef __attribute__((ext_vector_type(2))) float f32x2;
typedef __attribute__((ext_vector_type(4))) float f32x4;
typedef __attribute__((ext_vector_type(16))) float f32x16;
typedef __attribute__((ext_vector_type(4))) unsigned u32x4;
typedef __attribute__((address_space(3))) bf16x4* lds_x4_ptr;

#define DI __device__ __forceinline__

constexpr int S_ = 2048, D_ = 640, H_ = 32, DH_ = 20, DF_ = 1728;
constexpr float LN_EPS = 1e-5f;

DI unsigned pack2(float lo, float hi) {
  f32x2 v = {lo, hi};
  bf2_t r = __builtin_convertvector(v, bf2_t);
  return __builtin_bit_cast(unsigned, r);
}
DI float bf_lo(unsigned u) { return __uint_as_float(u << 16); }
DI float bf_hi(unsigned u) { return __uint_as_float(u & 0xffff0000u); }
DI float dot2_bf16(unsigned a, unsigned b, float c) {
  return __builtin_amdgcn_fdot2_f32_bf16(__builtin_bit_cast(bf2_t, a), __builtin_bit_cast(bf2_t, b), c, false);
}
DI void glds16(const void* g, void* l) {
  __builtin_amdgcn_global_load_lds((const unsigned*)g, (unsigned*)l, 16, 0, 0);
}
#define WAIT_VM(n) asm volatile("s_waitcnt vmcnt(" #n ")" ::: "memory")
#define RAW_BAR() __builtin_amdgcn_s_barrier()

template <int MT, int NT, int NMAT, int WM, int WN, int STAGES>
struct GemmCfg {
  static constexpr int NW = WM * WN;
  static constexpr int BM = 16 * MT * WM;
  static constexpr int BN = 16 * NT * WN;
  static constexpr int NU = BN / 16;
  static constexpr int XBYTES = BM * 128;
  static constexpr int WBYTES = 64 * BN * 2;
  static constexpr int STAGE_BYTES = XBYTES + NMAT * WBYTES;
  static constexpr int XI = XBYTES / 1024;
  static constexpr int WI = NMAT * WBYTES / 1024;
  static constexpr int TOT = XI + WI;
  static constexpr int G = (TOT + NW - 1) / NW;
  static constexpr int LDS_BYTES = STAGES * STAGE_BYTES;
};

template <int NU>
DI void wimg_decode(int g, int& krow, int& cg) {
  int b = g >> 4, rs = (g & 15) >> 1, c = g & 1;
  int koct = b / NU, nunit = b % NU;
  krow = koct * 8 + (rs ^ ((koct & 1) << 2));
  cg = nunit * 2 + c;
}

template <int MT, int NT, int NMAT, int WM, int WN, int STAGES, bool STATS>
DI void gemm_core(char* smem, const char* (&src)[GemmCfg<MT, NT, NMAT, WM, WN, STAGES>::G],
                  const int (&sstride)[GemmCfg<MT, NT, NMAT, WM, WN, STAGES>::G], int ktiles, int wave,
                  int lane, f32x4 (&acc)[NMAT][NT][MT], float (&s1)[MT], float (&s2)[MT],
                  float (&cs)[NMAT][NT]) {
  using C = GemmCfg<MT, NT, NMAT, WM, WN, STAGES>;
  constexpr int G = C::G, NU = C::NU;
  const int wm = wave / WN, wn = wave % WN;

  const int xr = lane & 15, xq = lane >> 4;
  const int xbase0 = xr * 128 + (((0 + xq) ^ ((xr >> 1) & 7)) * 16) + wm * MT * 2048;
  const int xbase1 = xr * 128 + (((4 + xq) ^ ((xr >> 1) & 7)) * 16) + wm * MT * 2048;
  const int wg = lane >> 4, wq = (lane & 15) >> 2, wp = lane & 3;
  const int wbase_lo = C::XBYTES + wg * NU * 256 + ((wq ^ (4 * (wg & 1))) * 32) + wp * 8 + wn * NT * 256;
  const int wbase_hi = wbase_lo ^ 128;

  int ldsoff[G];
#pragma unroll
  for (int j = 0; j < G; ++j) {
    int i = wave + j * C::NW;
    if (i >= C::TOT) i = i - C::NW * ((i - C::TOT) / C::NW + 1);
    ldsoff[j] = i * 1024 + lane * 16;
  }

  auto issue = [&](int buf) {
#pragma unroll
    for (int j = 0; j < G; ++j) {
      glds16(src[j], smem + buf * C::STAGE_BYTES + ldsoff[j]);
      src[j] += sstride[j];
    }
  };

#pragma unroll
  for (int s = 0; s < STAGES - 1; ++s) issue(s);

  int rbuf = 0, wbuf = STAGES - 1;
  for (int kt = 0; kt < ktiles; ++kt) {
    {
      constexpr int INFL = (STAGES - 2) * G;
      if (kt + STAGES - 2 <= ktiles - 1) {
        asm volatile("s_waitcnt vmcnt(%0)" ::"n"(INFL) : "memory");
      } else {
        asm volatile("s_waitcnt vmcnt(0)" ::: "memory");
      }
    }
    RAW_BAR();
    if (kt + STAGES - 1 < ktiles) issue(wbuf);
    const char* sb = smem + rbuf * C::STAGE_BYTES;
#pragma unroll
    for (int ks = 0; ks < 2; ++ks) {
      bf16x8 xf[MT];
#pragma unroll
      for (int mt = 0; mt < MT; ++mt) xf[mt] = *(const bf16x8*)(sb + (ks ? xbase1 : xbase0) + mt * 2048);
      bf16x8 wf[NMAT][NT];
#pragma unroll
      for (int mat = 0; mat < NMAT; ++mat)
#pragma unroll
        for (int nt = 0; nt < NT; ++nt) {
          const int off = mat * C::WBYTES + ks * 4 * NU * 256 + nt * 256;
          bf16x4 lo = __builtin_amdgcn_ds_read_tr16_b64_v4i16((lds_x4_ptr)(sb + wbase_lo + off));
          bf16x4 hi = __builtin_amdgcn_ds_read_tr16_b64_v4i16((lds_x4_ptr)(sb + wbase_hi + off));
          wf[mat][nt] = __builtin_shufflevector(lo, hi, 0, 1, 2, 3, 4, 5, 6, 7);
        }
#pragma unroll
      for (int mat = 0; mat < NMAT; ++mat)
#pragma unroll
        for (int nt = 0; nt < NT; ++nt)
#pragma unroll
          for (int mt = 0; mt < MT; ++mt)
            acc[mat][nt][mt] = __builtin_amdgcn_mfma_f32_16x16x32_bf16(wf[mat][nt], xf[mt], acc[mat][nt][mt], 0, 0, 0);
      if constexpr (STATS) {
#pragma unroll
        for (int mt = 0; mt < MT; ++mt) {
          u32x4 u = __builtin_bit_cast(u32x4, xf[mt]);
#pragma unroll
          for (int d = 0; d < 4; ++d) {
            s1[mt] = dot2_bf16(u[d], 0x3f803f80u, s1[mt]);
            s2[mt] = dot2_bf16(u[d], u[d], s2[mt]);
          }
        }
#pragma unroll
        for (int mat = 0; mat < NMAT; ++mat)
#pragma unroll
          for (int nt = 0; nt < NT; ++nt) {
            u32x4 u = __builtin_bit_cast(u32x4, wf[mat][nt]);
#pragma unroll
            for (int d = 0; d < 4; ++d) cs[mat][nt] = dot2_bf16(u[d], 0x3f803f80u, cs[mat][nt]);
          }
      }
    }
    rbuf = (rbuf + 1 == STAGES) ? 0 : rbuf + 1;
    wbuf = (wbuf + 1 == STAGES) ? 0 : wbuf + 1;
  }
}

DI float red4(float v) {
  v += __shfl_xor(v, 16);
  v += __shfl_xor(v, 32);
  return v;
}

constexpr size_t WS_X1 = 0;
constexpr size_t WS_X2 = WS_X1 + (size_t)S_ * D_ * 2;
constexpr size_t WS_X3 = WS_X2 + (size_t)S_ * D_ * 2;
constexpr size_t WS_Q = WS_X3 + (size_t)S_ * D_ * 2;
constexpr size_t WS_O = WS_Q + (size_t)S_ * D_ * 2;
constexpr size_t WS_KA = WS_O + (size_t)S_ * D_ * 2;
constexpr size_t WS_KB = WS_KA + (size_t)S_ * 8 * 2;
constexpr size_t WS_KC = WS_KB + (size_t)S_ * 8 * 2;
constexpr size_t WS_VT = WS_KC + (size_t)S_ * 4 * 2;
constexpr size_t WS_A = WS_VT + (size_t)DH_ * S_ * 2;
constexpr size_t WS_END = WS_A + (size_t)S_ * DF_ * 2;

using CfgS = GemmCfg<1, 5, 1, 4, 1, 4>;

__global__ __launch_bounds__(256) void k_qkv(const u16* __restrict__ x, const u16* __restrict__ wq,
                                             const u16* __restrict__ wk, const u16* __restrict__ wv,
                                             char* __restrict__ ws) {
  extern __shared__ __attribute__((aligned(1024))) char smem[];
  using C = CfgS;
  constexpr int G = C::G;
  const int tid = threadIdx.x, lane = tid & 63, wave = __builtin_amdgcn_readfirstlane(tid >> 6);
  const int bid = blockIdx.x, xcd = bid & 7, idx = bid >> 3;
  const int mtile = xcd * 4 + (idx & 3), ntile = idx >> 2;
  const int m0 = mtile * 64, n0 = ntile * 80;
  const bool kv = (ntile == 8);

  const char* src[G];
  int sstride[G];
#pragma unroll
  for (int j = 0; j < G; ++j) {
    int i = wave + j * C::NW;
    if (i >= C::TOT) i = i - C::NW * ((i - C::TOT) / C::NW + 1);
    if (i < C::XI) {
      int g = i * 64 + lane, row = g >> 3, chunk = (g & 7) ^ ((row >> 1) & 7);
      src[j] = (const char*)(x + (size_t)(m0 + row) * D_ + chunk * 8);
      sstride[j] = 128;
    } else {
      int g = (i - C::XI) * 64 + lane, krow, cg;
      wimg_decode<C::NU>(g, krow, cg);
      if (!kv) {
        src[j] = (const char*)(wq + (size_t)krow * D_ + n0 + cg * 8);
        sstride[j] = 64 * D_ * 2;
      } else {
        const u16* wsrc = (cg >= 3 && cg < 6) ? wv : wk;
        int c3 = (cg < 6) ? (cg % 3) : 0;
        int coff = (c3 == 2) ? 12 : c3 * 8;
        src[j] = (const char*)(wsrc + (size_t)krow * DH_ + coff);
        sstride[j] = 64 * DH_ * 2;
      }
    }
  }

  f32x4 acc[1][5][1];
  float s1[1] = {0.f}, s2[1] = {0.f}, cs[1][5];
#pragma unroll
  for (int n = 0; n < 5; ++n) { acc[0][n][0] = f32x4{0.f, 0.f, 0.f, 0.f}; cs[0][n] = 0.f; }
  gemm_core<1, 5, 1, 4, 1, 4, true>(smem, src, sstride, D_ / 64, wave, lane, acc, s1, s2, cs);

  const float sum = red4(s1[0]), sq = red4(s2[0]);
  const float mean = sum * (1.f / D_);
  const float var = fmaxf(sq * (1.f / D_) - mean * mean, 0.f);
  const float rstd = rsqrtf(var + LN_EPS);
  const int token = m0 + wave * 16 + (lane & 15);
  const int quad = lane >> 4;
  float v[5][4];
#pragma unroll
  for (int u = 0; u < 5; ++u) {
    float c = red4(cs[0][u]);
#pragma unroll
    for (int r = 0; r < 4; ++r) {
      float cn = __shfl(c, quad * 4 + r);
      v[u][r] = rstd * (acc[0][u][0][r] - mean * cn);
    }
  }
  if (!kv) {
    const float qs = 0.22360679774997896f * 1.4426950408889634f;
    u16* q = (u16*)(ws + WS_Q) + (size_t)token * D_ + n0 + quad * 4;
#pragma unroll
    for (int u = 0; u < 5; ++u) {
      uint2 o;
      o.x = pack2(v[u][0] * qs, v[u][1] * qs);
      o.y = pack2(v[u][2] * qs, v[u][3] * qs);
      *(uint2*)(q + u * 16) = o;
    }
  } else {
    u16* ka = (u16*)(ws + WS_KA);
    u16* kb = (u16*)(ws + WS_KB);
    u16* kc = (u16*)(ws + WS_KC);
    u16* vt = (u16*)(ws + WS_VT);
    {
      uint2 o;
      o.x = pack2(v[0][0], v[0][1]);
      o.y = pack2(v[0][2], v[0][3]);
      u16* dst = (quad < 2 ? ka : kb) + (size_t)token * 8 + (quad & 1) * 4;
      *(uint2*)dst = o;
    }
    const int o16 = token & 15;
    const int pos = (token & ~15) + 8 * ((o16 >> 2) & 1) + 4 * (o16 >> 3) + (o16 & 3);
    if (quad == 1) {
      uint2 o;
      o.x = pack2(v[1][0], v[1][1]);
      o.y = pack2(v[1][2], v[1][3]);
      *(uint2*)(kc + (size_t)token * 4) = o;
    } else if (quad >= 2) {
      int d0 = (quad - 2) * 4;
#pragma unroll
      for (int r = 0; r < 4; ++r) {
        unsigned p = pack2(v[1][r], 0.f);
        vt[(size_t)(d0 + r) * S_ + pos] = (u16)(p & 0xffff);
      }
    }
    if (quad != 2) {
      int d0 = (quad == 3) ? 16 : 8 + quad * 4;
#pragma unroll
      for (int r = 0; r < 4; ++r) {
        unsigned p = pack2(v[2][r], 0.f);
        vt[(size_t)(d0 + r) * S_ + pos] = (u16)(p & 0xffff);
      }
    }
  }
}

template <int KDIM>
__global__ __launch_bounds__(256) void k_proj_res(const u16* __restrict__ a, const u16* __restrict__ w,
                                                  const u16* __restrict__ res, u16* __restrict__ out) {
  extern __shared__ __attribute__((aligned(1024))) char smem[];
  using C = CfgS;
  constexpr int G = C::G;
  const int tid = threadIdx.x, lane = tid & 63, wave = __builtin_amdgcn_readfirstlane(tid >> 6);
  const int bid = blockIdx.x, xcd = bid & 7, idx = bid >> 3;
  const int mtile = xcd * 4 + (idx & 3), ntile = idx >> 2;
  const int m0 = mtile * 64, n0 = ntile * 80;

  const char* src[G];
  int sstride[G];
#pragma unroll
  for (int j = 0; j < G; ++j) {
    int i = wave + j * C::NW;
    if (i >= C::TOT) i = i - C::NW * ((i - C::TOT) / C::NW + 1);
    if (i < C::XI) {
      int g = i * 64 + lane, row = g >> 3, chunk = (g & 7) ^ ((row >> 1) & 7);
      src[j] = (const char*)(a + (size_t)(m0 + row) * KDIM + chunk * 8);
      sstride[j] = 128;
    } else {
      int g = (i - C::XI) * 64 + lane, krow, cg;
      wimg_decode<C::NU>(g, krow, cg);
      src[j] = (const char*)(w + (size_t)krow * D_ + n0 + cg * 8);
      sstride[j] = 64 * D_ * 2;
    }
  }
  f32x4 acc[1][5][1];
  float s1[1] = {0.f}, s2[1] = {0.f}, cs[1][5];
#pragma unroll
  for (int n = 0; n < 5; ++n) { acc[0][n][0] = f32x4{0.f, 0.f, 0.f, 0.f}; cs[0][n] = 0.f; }
  gemm_core<1, 5, 1, 4, 1, 4, false>(smem, src, sstride, KDIM / 64, wave, lane, acc, s1, s2, cs);

  const int token = m0 + wave * 16 + (lane & 15);
  const int quad = lane >> 4;
  const size_t base = (size_t)token * D_ + n0 + quad * 4;
#pragma unroll
  for (int u = 0; u < 5; ++u) {
    uint2 r = *(const uint2*)(res + base + u * 16);
    uint2 o;
    o.x = pack2(bf_lo(r.x) + acc[0][u][0][0], bf_hi(r.x) + acc[0][u][0][1]);
    o.y = pack2(bf_lo(r.y) + acc[0][u][0][2], bf_hi(r.y) + acc[0][u][0][3]);
    *(uint2*)(out + base + u * 16) = o;
  }
}

using CfgF = GemmCfg<4, 2, 2, 4, 2, 3>;

__global__ __launch_bounds__(512) void k_gateup(const u16* __restrict__ x, const u16* __restrict__ wg,
                                                const u16* __restrict__ wu, u16* __restrict__ aout) {
  extern __shared__ __attribute__((aligned(1024))) char smem[];
  using C = CfgF;
  constexpr int G = C::G;
  const int tid = threadIdx.x, lane = tid & 63, wave = __builtin_amdgcn_readfirstlane(tid >> 6);
  const int bid = blockIdx.x;
  const int mtile = bid & 7, ntile = bid >> 3;
  const int m0 = mtile * 256, n0 = ntile * 64;

  const char* src[G];
  int sstride[G];
#pragma unroll
  for (int j = 0; j < G; ++j) {
    int i = wave + j * C::NW;
    if (i < C::XI) {
      int g = i * 64 + lane, row = g >> 3, chunk = (g & 7) ^ ((row >> 1) & 7);
      src[j] = (const char*)(x + (size_t)(m0 + row) * D_ + chunk * 8);
      sstride[j] = 128;
    } else {
      int ii = i - C::XI;
      constexpr int PER = C::WBYTES / 1024;
      int mat = ii / PER;
      int g = (ii % PER) * 64 + lane, krow, cg;
      wimg_decode<C::NU>(g, krow, cg);
      const u16* w = mat ? wu : wg;
      src[j] = (const char*)(w + (size_t)krow * DF_ + n0 + cg * 8);
      sstride[j] = 64 * DF_ * 2;
    }
  }
  f32x4 acc[2][2][4];
  float s1[4], s2[4], cs[2][2];
#pragma unroll
  for (int m = 0; m < 4; ++m) { s1[m] = 0.f; s2[m] = 0.f; }
#pragma unroll
  for (int a = 0; a < 2; ++a)
#pragma unroll
    for (int n = 0; n < 2; ++n) {
      cs[a][n] = 0.f;
#pragma unroll
      for (int m = 0; m < 4; ++m) acc[a][n][m] = f32x4{0.f, 0.f, 0.f, 0.f};
    }
  gemm_core<4, 2, 2, 4, 2, 3, true>(smem, src, sstride, D_ / 64, wave, lane, acc, s1, s2, cs);

  const int wm = wave >> 1, wn = wave & 1;
  const int quad = lane >> 4;
  float cg_[2][4], cu_[2][4];
#pragma unroll
  for (int n = 0; n < 2; ++n) {
    float a = red4(cs[0][n]), b = red4(cs[1][n]);
#pragma unroll
    for (int r = 0; r < 4; ++r) {
      cg_[n][r] = __shfl(a, quad * 4 + r);
      cu_[n][r] = __shfl(b, quad * 4 + r);
    }
  }
#pragma unroll
  for (int mt = 0; mt < 4; ++mt) {
    const float sum = red4(s1[mt]), sq = red4(s2[mt]);
    const float mean = sum * (1.f / D_);
    const float var = fmaxf(sq * (1.f / D_) - mean * mean, 0.f);
    const float rstd = rsqrtf(var + LN_EPS);
    const int token = m0 + (wm * 4 + mt) * 16 + (lane & 15);
#pragma unroll
    for (int n = 0; n < 2; ++n) {
      float o[4];
#pragma unroll
      for (int r = 0; r < 4; ++r) {
        float g = rstd * (acc[0][n][mt][r] - mean * cg_[n][r]);
        float u = rstd * (acc[1][n][mt][r] - mean * cu_[n][r]);
        float y = g * (1.0f + 0.044715f * g * g);
        float e = __builtin_amdgcn_exp2f(y * (-2.0f * 0.7978845608028654f * 1.4426950408889634f));
        o[r] = g * __builtin_amdgcn_rcpf(1.0f + e) * u;
      }
      uint2 ov;
      ov.x = pack2(o[0], o[1]);
      ov.y = pack2(o[2], o[3]);
      *(uint2*)(aout + (size_t)token * DF_ + n0 + (wn * 2 + n) * 16 + quad * 4) = ov;
    }
  }
}

constexpr int AT_CHUNK = 512;
constexpr int AT_KA = 0, AT_KB = AT_CHUNK * 16, AT_KC = 2 * AT_CHUNK * 16;
constexpr int AT_VT = AT_KC + AT_CHUNK * 8;
constexpr int AT_VROW = AT_CHUNK * 2 + 16;
constexpr int AT_BUF = AT_VT + 21 * AT_VROW;
constexpr int AT_LDS = 2 * AT_BUF;

DI float other_half_max(float x) {
  auto r = __builtin_amdgcn_permlane32_swap(__float_as_uint(x), __float_as_uint(x), false, false);
  return fmaxf(__uint_as_float(r[0]), __uint_as_float(r[1]));
}

__global__ __launch_bounds__(512, 2) void k_attn(const char* __restrict__ ws_in, u16* __restrict__ oout) {
  extern __shared__ __attribute__((aligned(1024))) char smem[];
  const int tid = threadIdx.x, lane = tid & 63, wave = __builtin_amdgcn_readfirstlane(tid >> 6);
  const int h = lane >> 5, c = lane & 31;
  const int token = blockIdx.x * 8 + wave;

  const char* src[5];
  int ldso[5], sstr[5];
#pragma unroll
  for (int j = 0; j < 5; ++j) {
    int i = wave + j * 8;
    if (i < 8) {
      src[j] = ws_in + WS_KA + i * 1024 + lane * 16; ldso[j] = AT_KA + i * 1024; sstr[j] = AT_CHUNK * 16;
    } else if (i < 16) {
      src[j] = ws_in + WS_KB + (i - 8) * 1024 + lane * 16; ldso[j] = AT_KB + (i - 8) * 1024; sstr[j] = AT_CHUNK * 16;
    } else if (i < 20) {
      src[j] = ws_in + WS_KC + (i - 16) * 1024 + lane * 16; ldso[j] = AT_KC + (i - 16) * 1024; sstr[j] = AT_CHUNK * 8;
    } else {
      int d = i - 20;
      src[j] = ws_in + WS_VT + (size_t)d * S_ * 2 + lane * 16; ldso[j] = AT_VT + d * AT_VROW; sstr[j] = AT_CHUNK * 2;
    }
  }
  auto issue = [&](int buf) {
#pragma unroll
    for (int j = 0; j < 5; ++j) {
      glds16(src[j], smem + buf * AT_BUF + ldso[j] + lane * 16);
      src[j] += sstr[j];
    }
  };
  issue(0);

  if (tid < 128) {
    int b = tid >> 6;
    *(u32x4*)(smem + b * AT_BUF + AT_VT + 20 * AT_VROW + lane * 16) = u32x4{0x3f803f80u, 0x3f803f80u, 0x3f803f80u, 0x3f803f80u};
  }

  bf16x8 qf0, qf1;
  {
    const u16* q = (const u16*)(ws_in + WS_Q) + (size_t)token * D_ + c * DH_;
    uint2 a = *(const uint2*)(q + 8 * h), b = *(const uint2*)(q + 8 * h + 4);
    qf0 = __builtin_bit_cast(bf16x8, u32x4{a.x, a.y, b.x, b.y});
    uint2 t = *(const uint2*)(q + 16);
    if (h) t = uint2{0u, 0u};
    qf1 = __builtin_bit_cast(bf16x8, u32x4{t.x, t.y, 0u, 0u});
  }

  f32x16 O, Mb;
#pragma unroll
  for (int i = 0; i < 16; ++i) { O[i] = 0.f; Mb[i] = 0.f; }
  float m = 0.f;
  bool first = true;

  const int kbase = (h ? AT_KB : AT_KA) + c * 16;
  const int kcbase = AT_KC + c * 8;
  const int vrow = (c < 20 ? c : 20);
  const int vbase = AT_VT + vrow * AT_VROW + h * 16;

  for (int ch = 0; ch < S_ / AT_CHUNK; ++ch) {
    asm volatile("s_waitcnt vmcnt(0) lgkmcnt(0)" ::: "memory");
    RAW_BAR();
    if (ch + 1 < S_ / AT_CHUNK) issue((ch + 1) & 1);
    const char* sb = smem + (ch & 1) * AT_BUF;
    for (int st = 0; st < AT_CHUNK / 64; ++st) {
      f32x16 Sx[2];
      bf16x8 vf[2][2];
#pragma unroll
      for (int t = 0; t < 2; ++t) {
        const int key0 = st * 64 + t * 32;
        bf16x8 kf0 = *(const bf16x8*)(sb + kbase + key0 * 16);
        uint2 kc = *(const uint2*)(sb + kcbase + key0 * 8);
        bf16x8 kf1 = __builtin_bit_cast(bf16x8, u32x4{kc.x, kc.y, 0u, 0u});
        vf[t][0] = *(const bf16x8*)(sb + vbase + key0 * 2);
        vf[t][1] = *(const bf16x8*)(sb + vbase + key0 * 2 + 32);
        Sx[t] = __builtin_amdgcn_mfma_f32_32x32x16_bf16(kf0, qf0, Mb, 0, 0, 0);
        Sx[t] = __builtin_amdgcn_mfma_f32_32x32x16_bf16(kf1, qf1, Sx[t], 0, 0, 0);
      }
      float mx = fmaxf(Sx[0][0], Sx[0][1]);
#pragma unroll
      for (int i = 2; i < 16; ++i) mx = fmaxf(mx, Sx[0][i]);
#pragma unroll
      for (int i = 0; i < 16; ++i) mx = fmaxf(mx, Sx[1][i]);
      mx = other_half_max(mx);
      if (first || __any(mx > 0.f)) {
        float d = first ? mx : fmaxf(mx, 0.f);
        m += d;
#pragma unroll
        for (int i = 0; i < 16; ++i) { Mb[i] = -m; Sx[0][i] -= d; Sx[1][i] -= d; }
        if (!first) {
          float f = __builtin_amdgcn_exp2f(-d);
#pragma unroll
          for (int i = 0; i < 16; ++i) O[i] *= f;
        }
        first = false;
      }
#pragma unroll
      for (int t = 0; t < 2; ++t) {
#pragma unroll
        for (int s = 0; s < 2; ++s) {
          u32x4 pk;
#pragma unroll
          for (int j = 0; j < 4; ++j)
            pk[j] = pack2(__builtin_amdgcn_exp2f(Sx[t][8 * s + 2 * j]), __builtin_amdgcn_exp2f(Sx[t][8 * s + 2 * j + 1]));
          O = __builtin_amdgcn_mfma_f32_32x32x16_bf16(vf[t][s], __builtin_bit_cast(bf16x8, pk), O, 0, 0, 0);
        }
      }
    }
  }

  auto rr = __builtin_amdgcn_permlane32_swap(__float_as_uint(O[8]), __float_as_uint(O[8]), false, false);
  const float inv = 1.0f / __uint_as_float(rr[1]);
  u16* o = oout + (size_t)token * D_ + c * DH_;
  {
    uint2 v;
    v.x = pack2(O[0] * inv, O[1] * inv); v.y = pack2(O[2] * inv, O[3] * inv);
    *(uint2*)(o + 4 * h) = v;
    v.x = pack2(O[4] * inv, O[5] * inv); v.y = pack2(O[6] * inv, O[7] * inv);
    *(uint2*)(o + 8 + 4 * h) = v;
    if (h == 0) {
      v.x = pack2(O[8] * inv, O[9] * inv); v.y = pack2(O[10] * inv, O[11] * inv);
      *(uint2*)(o + 16) = v;
    }
  }
}

extern "C" void kernel_launch(void* const* d_in, const int* in_sizes, int n_in, void* d_out, int out_size,
                              void* d_ws, size_t ws_size, hipStream_t stream) {
  (void)in_sizes; (void)n_in; (void)out_size; (void)ws_size;
  const u16* x0 = (const u16*)d_in[0];
  const u16* wq = (const u16*)d_in[1];
  const u16* wk = (const u16*)d_in[2];
  const u16* wv = (const u16*)d_in[3];
  const u16* wo = (const u16*)d_in[4];
  const u16* wg = (const u16*)d_in[6];
  const u16* wu = (const u16*)d_in[7];
  const u16* wd = (const u16*)d_in[8];
  char* ws = (char*)d_ws;
  u16* out = (u16*)d_out;

  static bool attr_done = false;
  if (!attr_done) {
    hipFuncSetAttribute((const void*)k_qkv, hipFuncAttributeMaxDynamicSharedMemorySize, CfgS::LDS_BYTES);
    hipFuncSetAttribute((const void*)k_proj_res<D_>, hipFuncAttributeMaxDynamicSharedMemorySize, CfgS::LDS_BYTES);
    hipFuncSetAttribute((const void*)k_proj_res<DF_>, hipFuncAttributeMaxDynamicSharedMemorySize, CfgS::LDS_BYTES);
    hipFuncSetAttribute((const void*)k_gateup, hipFuncAttributeMaxDynamicSharedMemorySize, CfgF::LDS_BYTES);
    hipFuncSetAttribute((const void*)k_attn, hipFuncAttributeMaxDynamicSharedMemorySize, AT_LDS);
    attr_done = true;
  }

  const u16* xin = x0;
  for (int layer = 0; layer < 2; ++layer) {
    u16* x1 = (u16*)(ws + WS_X1);
    u16* x2 = (layer == 1) ? out : (u16*)(ws + WS_X2);
    k_qkv<<<288, 256, CfgS::LDS_BYTES, stream>>>(xin, wq, wk, wv, ws);
    k_attn<<<256, 512, AT_LDS, stream>>>(ws, (u16*)(ws + WS_O));
    k_proj_res<D_><<<256, 256, CfgS::LDS_BYTES, stream>>>((const u16*)(ws + WS_O), wo, xin, x1);
    k_gateup<<<216, 512, CfgF::LDS_BYTES, stream>>>(x1, wg, wu, (u16*)(ws + WS_A));
    k_proj_res<DF_><<<256, 256, CfgS::LDS_BYTES, stream>>>((const u16*)(ws + WS_A), wd, x1, x2);
    xin = x2;
  }
}
```

```cpp
#include <hip/hip_runtime.h>
#include <hip/hip_bf16.h>
#include <stdint.h>
#include <utility>
#include <type_traits>

typedef unsigned short u16;
typedef __attribute__((ext_vector_type(8))) short bf16x8;
typedef __attribute__((ext_vector_type(4))) short bf16x4;
typedef __attribute__((ext_vector_type(2))) __bf16 bf2_t;
typedef __attribute__((ext_vector_type(2))) float f32x2;
typedef __attribute__((ext_vector_type(4))) float f32x4;
typedef __attribute__((ext_vector_type(16))) float f32x16;
typedef __attribute__((ext_vector_type(4))) unsigned u32x4;
typedef __attribute__((address_space(3))) bf16x4* lds_x4_ptr;

#define DI __device__ __forceinline__

constexpr int S_ = 2048, D_ = 640, H_ = 32, DH_ = 20, DF_ = 1728;
constexpr float LN_EPS = 1e-5f;

DI unsigned pack2(float lo, float hi) {
  f32x2 v = {lo, hi};
  bf2_t r = __builtin_convertvector(v, bf2_t);
  return __builtin_bit_cast(unsigned, r);
}
DI float bf_lo(unsigned u) { return __uint_as_float(u << 16); }
DI float bf_hi(unsigned u) { return __uint_as_float(u & 0xffff0000u); }
DI float dot2_bf16(unsigned a, unsigned b, float c) {
  return __builtin_amdgcn_fdot2_f32_bf16(__builtin_bit_cast(bf2_t, a), __builtin_bit_cast(bf2_t, b), c, false);
}
DI void glds16(const void* g, void* l) {
  __builtin_amdgcn_global_load_lds((const unsigned*)g, (unsigned*)l, 16, 0, 0);
}
#define RAW_BAR() __builtin_amdgcn_s_barrier()

template <class F, int... I>
DI void static_for_impl(F& f, std::integer_sequence<int, I...>) { (f(std::integral_constant<int, I>{}), ...); }
template <int N, class F>
DI void static_for(F&& f) { static_for_impl(f, std::make_integer_sequence<int, N>{}); }

template <int OFF>
DI void lds_rd128(bf16x8& d, unsigned addr) {
  asm volatile("ds_read_b128 %0, %1 offset:%2" : "=&v"(d) : "v"(addr), "i"(OFF));
}
template <int OFF>
DI void lds_rd64tr(bf16x4& d, unsigned addr) {
  asm volatile("ds_read_b64_tr_b16 %0, %1 offset:%2" : "=&v"(d) : "v"(addr), "i"(OFF));
}
DI unsigned lds_addr(const void* p) { return (unsigned)(size_t)p; }
DI void pin(bf16x8& a) { asm volatile("" : "+v"(a)); }
DI void pin(bf16x4& a, bf16x4& b) { asm volatile("" : "+v"(a), "+v"(b)); }

template <int MT, int NT, int NMAT, int WM, int WN, int STAGES>
struct GemmCfg {
  static constexpr int NW = WM * WN;
  static constexpr int NL = 4;
  static constexpr int THREADS = (NW + NL) * 64;
  static constexpr int BM = 16 * MT * WM;
  static constexpr int BN = 16 * NT * WN;
  static constexpr int NU = BN / 16;
  static constexpr int XBYTES = BM * 128;
  static constexpr int WBYTES = 64 * BN * 2;
  static constexpr int STAGE_BYTES = XBYTES + NMAT * WBYTES;
  static constexpr int XI = XBYTES / 1024;
  static constexpr int WI = NMAT * WBYTES / 1024;
  static constexpr int TOT = XI + WI;
  static constexpr int G = (TOT + NL - 1) / NL;
  static constexpr int LDS_BYTES = STAGES * STAGE_BYTES;
};

template <int NU>
DI void wimg_decode(int g, int& krow, int& cg) {
  int b = g >> 4, rs = (g & 15) >> 1, c = g & 1;
  int koct = b / NU, nunit = b % NU;
  krow = koct * 8 + (rs ^ ((koct & 1) << 2));
  cg = nunit * 2 + c;
}

template <class C>
DI int dma_index(int lw, int j) {
  int i = lw + j * C::NL;
  if (i >= C::TOT) i -= C::NL * ((i - C::TOT) / C::NL + 1);
  return i;
}

template <int MT, int NT, int NMAT, int WM, int WN, int STAGES>
DI void gemm_loader(char* smem, const char* (&src)[GemmCfg<MT, NT, NMAT, WM, WN, STAGES>::G],
                    const int (&sstride)[GemmCfg<MT, NT, NMAT, WM, WN, STAGES>::G], int ktiles, int lw, int lane) {
  using C = GemmCfg<MT, NT, NMAT, WM, WN, STAGES>;
  constexpr int G = C::G;
  int ldsoff[G];
#pragma unroll
  for (int j = 0; j < G; ++j) ldsoff[j] = dma_index<C>(lw, j) * 1024 + lane * 16;
  auto issue = [&](int buf) {
#pragma unroll
    for (int j = 0; j < G; ++j) {
      glds16(src[j], smem + buf * C::STAGE_BYTES + ldsoff[j]);
      src[j] += sstride[j];
    }
  };
#pragma unroll
  for (int s = 0; s < STAGES - 1; ++s) issue(s);
  int wbuf = STAGES - 1;
  for (int kt = 0; kt < ktiles; ++kt) {
    constexpr int INFL = (STAGES - 2) * G;
    if (kt + STAGES - 2 <= ktiles - 1) {
      asm volatile("s_waitcnt vmcnt(%0)" ::"n"(INFL) : "memory");
    } else {
      asm volatile("s_waitcnt vmcnt(0)" ::: "memory");
    }
    RAW_BAR();
    if (kt + STAGES - 1 < ktiles) issue(wbuf);
    wbuf = (wbuf + 1 == STAGES) ? 0 : wbuf + 1;
  }
}

template <int MT, int NT, int NMAT, int WM, int WN, int STAGES, bool STATS>
DI void gemm_compute(char* smem, int ktiles, int wave, int lane, f32x4 (&acc)[NMAT][NT][MT], f32x4 (&s1)[MT],
                     f32x4 (&s2)[MT], f32x4 (&cs)[NMAT][NT]) {
  using C = GemmCfg<MT, NT, NMAT, WM, WN, STAGES>;
  constexpr int NU = C::NU;
  const int wm = wave / WN, wn = wave % WN;
  const int xr = lane & 15, xq = lane >> 4;
  const unsigned lbase = lds_addr(smem);
  const unsigned xbase0 = lbase + xr * 128 + (((0 + xq) ^ ((xr >> 1) & 7)) * 16) + wm * MT * 2048;
  const unsigned xbase1 = lbase + xr * 128 + (((4 + xq) ^ ((xr >> 1) & 7)) * 16) + wm * MT * 2048;
  const int wg = lane >> 4, wq = (lane & 15) >> 2, wp = lane & 3;
  const unsigned wbase_lo = lbase + C::XBYTES + wg * NU * 256 + ((wq ^ (4 * (wg & 1))) * 32) + wp * 8 + wn * NT * 256;
  const unsigned wbase_hi = wbase_lo ^ 128;
  const bf16x8 ones = __builtin_bit_cast(bf16x8, u32x4{0x3f803f80u, 0x3f803f80u, 0x3f803f80u, 0x3f803f80u});

  int rbuf = 0;
  for (int kt = 0; kt < ktiles; ++kt) {
    RAW_BAR();
    const unsigned so = rbuf * C::STAGE_BYTES;
    const unsigned xa0 = so + xbase0, xa1 = so + xbase1, wlo = so + wbase_lo, whi = so + wbase_hi;
    bf16x8 xf[2][MT];
    bf16x4 wl[2][NMAT][NT], wh[2][NMAT][NT];
    static_for<2>([&](auto KS) {
      constexpr int ks = KS.value;
      static_for<MT>([&](auto M) { lds_rd128<M.value * 2048>(xf[ks][M.value], ks ? xa1 : xa0); });
      static_for<NMAT>([&](auto MA) {
        static_for<NT>([&](auto N) {
          constexpr int off = MA.value * C::WBYTES + ks * 4 * NU * 256 + N.value * 256;
          lds_rd64tr<off>(wl[ks][MA.value][N.value], wlo);
          lds_rd64tr<off>(wh[ks][MA.value][N.value], whi);
        });
      });
    });
    static_for<2>([&](auto KS) {
      constexpr int ks = KS.value;
      constexpr int NLATER = (ks == 0) ? (MT + 2 * NMAT * NT) : 0;
      asm volatile("s_waitcnt lgkmcnt(%0)" ::"n"(NLATER) : "memory");
#pragma unroll
      for (int mt = 0; mt < MT; ++mt) pin(xf[ks][mt]);
#pragma unroll
      for (int mat = 0; mat < NMAT; ++mat)
#pragma unroll
        for (int nt = 0; nt < NT; ++nt) pin(wl[ks][mat][nt], wh[ks][mat][nt]);
      bf16x8 wf[NMAT][NT];
#pragma unroll
      for (int mat = 0; mat < NMAT; ++mat)
#pragma unroll
        for (int nt = 0; nt < NT; ++nt)
          wf[mat][nt] = __builtin_shufflevector(wl[ks][mat][nt], wh[ks][mat][nt], 0, 1, 2, 3, 4, 5, 6, 7);
#pragma unroll
      for (int mat = 0; mat < NMAT; ++mat)
#pragma unroll
        for (int nt = 0; nt < NT; ++nt)
#pragma unroll
          for (int mt = 0; mt < MT; ++mt)
            acc[mat][nt][mt] = __builtin_amdgcn_mfma_f32_16x16x32_bf16(wf[mat][nt], xf[ks][mt], acc[mat][nt][mt], 0, 0, 0);
      if constexpr (STATS) {
#pragma unroll
        for (int mt = 0; mt < MT; ++mt) {
          s1[mt] = __builtin_amdgcn_mfma_f32_16x16x32_bf16(ones, xf[ks][mt], s1[mt], 0, 0, 0);
          s2[mt] = __builtin_amdgcn_mfma_f32_16x16x32_bf16(xf[ks][mt], xf[ks][mt], s2[mt], 0, 0, 0);
        }
#pragma unroll
        for (int mat = 0; mat < NMAT; ++mat)
#pragma unroll
          for (int nt = 0; nt < NT; ++nt)
            cs[mat][nt] = __builtin_amdgcn_mfma_f32_16x16x32_bf16(wf[mat][nt], ones, cs[mat][nt], 0, 0, 0);
      }
    });
    rbuf = (rbuf + 1 == STAGES) ? 0 : rbuf + 1;
  }
}

DI float red4(float v) {
  v += __shfl_xor(v, 16);
  v += __shfl_xor(v, 32);
  return v;
}

constexpr size_t WS_X1 = 0;
constexpr size_t WS_X2 = WS_X1 + (size_t)S_ * D_ * 2;
constexpr size_t WS_X3 = WS_X2 + (size_t)S_ * D_ * 2;
constexpr size_t WS_Q = WS_X3 + (size_t)S_ * D_ * 2;
constexpr size_t WS_O = WS_Q + (size_t)S_ * D_ * 2;
constexpr size_t WS_KA = WS_O + (size_t)S_ * D_ * 2;
constexpr size_t WS_KB = WS_KA + (size_t)S_ * 8 * 2;
constexpr size_t WS_KC = WS_KB + (size_t)S_ * 8 * 2;
constexpr size_t WS_VT = WS_KC + (size_t)S_ * 8 * 2;
constexpr size_t WS_A = WS_VT + (size_t)DH_ * S_ * 2;
constexpr size_t WS_ST = WS_A + (size_t)S_ * DF_ * 2;
constexpr size_t WS_CS = WS_ST + (size_t)S_ * 8 * 8;
constexpr size_t WS_END = WS_CS + (size_t)2 * DF_ * 4;

using CfgS = GemmCfg<1, 5, 1, 4, 1, 4>;

__global__ __launch_bounds__(CfgS::THREADS) void k_qkv(const u16* __restrict__ x, const u16* __restrict__ wq,
                                             const u16* __restrict__ wk, const u16* __restrict__ wv,
                                             const u16* __restrict__ wg, const u16* __restrict__ wu,
                                             char* __restrict__ ws) {
  extern __shared__ __attribute__((aligned(1024))) char smem[];
  using C = CfgS;
  constexpr int G = C::G;
  const int tid = threadIdx.x, lane = tid & 63, wave = __builtin_amdgcn_readfirstlane(tid >> 6);
  if (blockIdx.x >= 288) {
    const int cb = blockIdx.x - 288;
    const u16* w = (cb < 27) ? wg : wu;
    const int col0 = (cb % 27) * 64;
    const int cp = tid & 31, rg = tid >> 5;
    float a0 = 0.f, a1 = 0.f;
    const unsigned* wp = (const unsigned*)(w + (size_t)(rg * 40) * DF_ + col0) + cp;
#pragma unroll 8
    for (int r = 0; r < 40; ++r) {
      unsigned v = wp[(size_t)r * (DF_ / 2)];
      a0 += bf_lo(v); a1 += bf_hi(v);
    }
    float* red = (float*)smem;
    red[(rg * 32 + cp) * 2] = a0; red[(rg * 32 + cp) * 2 + 1] = a1;
    __syncthreads();
    if (tid < 64) {
      float t = 0.f;
#pragma unroll
      for (int g = 0; g < 16; ++g) t += red[(g * 32 + (tid >> 1)) * 2 + (tid & 1)];
      ((float*)(ws + WS_CS))[(cb < 27 ? 0 : DF_) + col0 + tid] = t;
    }
    return;
  }
  const int bid = blockIdx.x, xcd = bid & 7, idx = bid >> 3;
  const int mtile = xcd * 4 + (idx & 3), ntile = idx >> 2;
  const int m0 = mtile * 64, n0 = ntile * 80;
  const bool kv = (ntile == 8);

  if (wave >= C::NW) {
    const int lw = wave - C::NW;
    const char* src[G];
    int sstride[G];
#pragma unroll
    for (int j = 0; j < G; ++j) {
      int i = dma_index<C>(lw, j);
      if (i < C::XI) {
        int g = i * 64 + lane, row = g >> 3, chunk = (g & 7) ^ ((row >> 1) & 7);
        src[j] = (const char*)(x + (size_t)(m0 + row) * D_ + chunk * 8);
        sstride[j] = 128;
      } else {
        int g = (i - C::XI) * 64 + lane, krow, cg;
        wimg_decode<C::NU>(g, krow, cg);
        if (!kv) {
          src[j] = (const char*)(wq + (size_t)krow * D_ + n0 + cg * 8);
          sstride[j] = 64 * D_ * 2;
        } else {
          const u16* wsrc = (cg >= 3 && cg < 6) ? wv : wk;
          int c3 = (cg < 6) ? (cg % 3) : 0;
          int coff = (c3 == 2) ? 12 : c3 * 8;
          src[j] = (const char*)(wsrc + (size_t)krow * DH_ + coff);
          sstride[j] = 64 * DH_ * 2;
        }
      }
    }
    gemm_loader<1, 5, 1, 4, 1, 4>(smem, src, sstride, D_ / 64, lw, lane);
    return;
  }

  f32x4 acc[1][5][1];
  f32x4 s1[1] = {f32x4{0.f, 0.f, 0.f, 0.f}}, s2[1] = {f32x4{0.f, 0.f, 0.f, 0.f}}, cs[1][5];
#pragma unroll
  for (int n = 0; n < 5; ++n) { acc[0][n][0] = f32x4{0.f, 0.f, 0.f, 0.f}; cs[0][n] = f32x4{0.f, 0.f, 0.f, 0.f}; }
  gemm_compute<1, 5, 1, 4, 1, 4, true>(smem, D_ / 64, wave, lane, acc, s1, s2, cs);

  const float sum = s1[0][0];
  const int l3 = lane & 3;
  const float dsel = l3 == 0 ? s2[0][0] : l3 == 1 ? s2[0][1] : l3 == 2 ? s2[0][2] : s2[0][3];
  const float sq = __shfl(dsel, 16 * ((lane & 15) >> 2) + (lane & 15));
  const float mean = sum * (1.f / D_);
  const float var = fmaxf(sq * (1.f / D_) - mean * mean, 0.f);
  const float rstd = rsqrtf(var + LN_EPS);
  const int token = m0 + wave * 16 + (lane & 15);
  const int quad = lane >> 4;
  float v[5][4];
#pragma unroll
  for (int u = 0; u < 5; ++u) {
#pragma unroll
    for (int r = 0; r < 4; ++r) v[u][r] = rstd * (acc[0][u][0][r] - mean * cs[0][u][r]);
  }
  if (!kv) {
    const float qs = 0.22360679774997896f * 1.4426950408889634f;
    u16* q = (u16*)(ws + WS_Q) + (size_t)token * D_ + n0 + quad * 4;
#pragma unroll
    for (int u = 0; u < 5; ++u) {
      uint2 o;
      o.x = pack2(v[u][0] * qs, v[u][1] * qs);
      o.y = pack2(v[u][2] * qs, v[u][3] * qs);
      *(uint2*)(q + u * 16) = o;
    }
  } else {
    u16* ka = (u16*)(ws + WS_KA);
    u16* kb = (u16*)(ws + WS_KB);
    u16* kc = (u16*)(ws + WS_KC);
    u16* vt = (u16*)(ws + WS_VT);
    {
      uint2 o;
      o.x = pack2(v[0][0], v[0][1]);
      o.y = pack2(v[0][2], v[0][3]);
      u16* dst = (quad < 2 ? ka : kb) + (size_t)token * 8 + (quad & 1) * 4;
      *(uint2*)dst = o;
    }
    const int o16 = token & 15;
    const int pos = (token & ~15) + 8 * ((o16 >> 2) & 1) + 4 * (o16 >> 3) + (o16 & 3);
    if (quad == 1) {
      u32x4 o;
      o[0] = pack2(v[1][0], v[1][1]);
      o[1] = pack2(v[1][2], v[1][3]);
      o[2] = 0u; o[3] = 0u;
      *(u32x4*)(kc + (size_t)token * 8) = o;
    } else if (quad >= 2) {
      int d0 = (quad - 2) * 4;
#pragma unroll
      for (int r = 0; r < 4; ++r) {
        unsigned p = pack2(v[1][r], 0.f);
        vt[(size_t)(d0 + r) * S_ + pos] = (u16)(p & 0xffff);
      }
    }
    if (quad != 2) {
      int d0 = (quad == 3) ? 16 : 8 + quad * 4;
#pragma unroll
      for (int r = 0; r < 4; ++r) {
        unsigned p = pack2(v[2][r], 0.f);
        vt[(size_t)(d0 + r) * S_ + pos] = (u16)(p & 0xffff);
      }
    }
  }
}

template <int KDIM>
__global__ __launch_bounds__(CfgS::THREADS) void k_proj_res(const u16* __restrict__ a, const u16* __restrict__ w,
                                                  const u16* __restrict__ res, u16* __restrict__ out,
                                                  float2* __restrict__ stats) {
  extern __shared__ __attribute__((aligned(1024))) char smem[];
  using C = CfgS;
  constexpr int G = C::G;
  const int tid = threadIdx.x, lane = tid & 63, wave = __builtin_amdgcn_readfirstlane(tid >> 6);
  const int bid = blockIdx.x, xcd = bid & 7, idx = bid >> 3;
  const int mtile = xcd * 4 + (idx & 3), ntile = idx >> 2;
  const int m0 = mtile * 64, n0 = ntile * 80;

  if (wave >= C::NW) {
    const int lw = wave - C::NW;
    const char* src[G];
    int sstride[G];
#pragma unroll
    for (int j = 0; j < G; ++j) {
      int i = dma_index<C>(lw, j);
      if (i < C::XI) {
        int g = i * 64 + lane, row = g >> 3, chunk = (g & 7) ^ ((row >> 1) & 7);
        src[j] = (const char*)(a + (size_t)(m0 + row) * KDIM + chunk * 8);
        sstride[j] = 128;
      } else {
        int g = (i - C::XI) * 64 + lane, krow, cg;
        wimg_decode<C::NU>(g, krow, cg);
        src[j] = (const char*)(w + (size_t)krow * D_ + n0 + cg * 8);
        sstride[j] = 64 * D_ * 2;
      }
    }
    gemm_loader<1, 5, 1, 4, 1, 4>(smem, src, sstride, KDIM / 64, lw, lane);
    return;
  }
  const int token = m0 + wave * 16 + (lane & 15);
  const int quad = lane >> 4;
  const size_t base = (size_t)token * D_ + n0 + quad * 4;
  uint2 rpre[5];
#pragma unroll
  for (int u = 0; u < 5; ++u) rpre[u] = *(const uint2*)(res + base + u * 16);

  f32x4 acc[1][5][1];
  f32x4 s1[1], s2[1], cs[1][5];
#pragma unroll
  for (int n = 0; n < 5; ++n) acc[0][n][0] = f32x4{0.f, 0.f, 0.f, 0.f};
  gemm_compute<1, 5, 1, 4, 1, 4, false>(smem, KDIM / 64, wave, lane, acc, s1, s2, cs);

  float ps = 0.f, pq = 0.f;
#pragma unroll
  for (int u = 0; u < 5; ++u) {
    uint2 r = rpre[u];
    uint2 o;
    o.x = pack2(bf_lo(r.x) + acc[0][u][0][0], bf_hi(r.x) + acc[0][u][0][1]);
    o.y = pack2(bf_lo(r.y) + acc[0][u][0][2], bf_hi(r.y) + acc[0][u][0][3]);
    *(uint2*)(out + base + u * 16) = o;
    float e0 = bf_lo(o.x), e1 = bf_hi(o.x), e2 = bf_lo(o.y), e3 = bf_hi(o.y);
    ps += (e0 + e1) + (e2 + e3);
    pq += (e0 * e0 + e1 * e1) + (e2 * e2 + e3 * e3);
  }
  if (stats != nullptr) {
    ps = red4(ps); pq = red4(pq);
    if (quad == 0) stats[(size_t)token * 8 + ntile] = float2{ps, pq};
  }
}

using CfgF = GemmCfg<4, 2, 2, 4, 2, 3>;

__global__ __launch_bounds__(CfgF::THREADS) void k_gateup(const u16* __restrict__ x, const u16* __restrict__ wg,
                                                const u16* __restrict__ wu, u16* __restrict__ aout,
                                                const char* __restrict__ ws) {
  extern __shared__ __attribute__((aligned(1024))) char smem[];
  using C = CfgF;
  constexpr int G = C::G;
  const int tid = threadIdx.x, lane = tid & 63, wave = __builtin_amdgcn_readfirstlane(tid >> 6);
  const int bid = blockIdx.x;
  const int xcd = bid & 7, idx = bid >> 3, pr = xcd >> 1, hf = xcd & 1;
  const int mtile = (idx < 26) ? 2 * pr + (idx & 1) : 2 * pr + hf;
  const int ntile = (idx < 26) ? hf * 14 + (idx >> 1) : 13;
  const int m0 = mtile * 256, n0 = ntile * 64;

  if (wave >= C::NW) {
    const int lw = wave - C::NW;
    const char* src[G];
    int sstride[G];
#pragma unroll
    for (int j = 0; j < G; ++j) {
      int i = dma_index<C>(lw, j);
      if (i < C::XI) {
        int g = i * 64 + lane, row = g >> 3, chunk = (g & 7) ^ ((row >> 1) & 7);
        src[j] = (const char*)(x + (size_t)(m0 + row) * D_ + chunk * 8);
        sstride[j] = 128;
      } else {
        int ii = i - C::XI;
        constexpr int PER = C::WBYTES / 1024;
        int mat = ii / PER;
        int g = (ii % PER) * 64 + lane, krow, cg;
        wimg_decode<C::NU>(g, krow, cg);
        const u16* w = mat ? wu : wg;
        src[j] = (const char*)(w + (size_t)krow * DF_ + n0 + cg * 8);
        sstride[j] = 64 * DF_ * 2;
      }
    }
    gemm_loader<4, 2, 2, 4, 2, 3>(smem, src, sstride, D_ / 64, lw, lane);
    return;
  }
  f32x4 acc[2][2][4];
  f32x4 s1[4], s2[4], cs[2][2];
#pragma unroll
  for (int a = 0; a < 2; ++a)
#pragma unroll
    for (int n = 0; n < 2; ++n)
#pragma unroll
      for (int m = 0; m < 4; ++m) acc[a][n][m] = f32x4{0.f, 0.f, 0.f, 0.f};
  gemm_compute<4, 2, 2, 4, 2, 3, false>(smem, D_ / 64, wave, lane, acc, s1, s2, cs);

  const int wm = wave >> 1, wn = wave & 1;
  const int quad = lane >> 4;
  float cg_[2][4], cu_[2][4];
  {
    const float* csg = (const float*)(ws + WS_CS) + n0 + wn * 32 + quad * 4;
#pragma unroll
    for (int n = 0; n < 2; ++n) {
      f32x4 a = *(const f32x4*)(csg + n * 16), b = *(const f32x4*)(csg + DF_ + n * 16);
#pragma unroll
      for (int r = 0; r < 4; ++r) { cg_[n][r] = a[r]; cu_[n][r] = b[r]; }
    }
  }
#pragma unroll
  for (int mt = 0; mt < 4; ++mt) {
    float sum = 0.f, sq = 0.f;
    {
      const f32x4* st = (const f32x4*)(ws + WS_ST) + (size_t)(m0 + (wm * 4 + mt) * 16 + (lane & 15)) * 4;
#pragma unroll
      for (int i = 0; i < 4; ++i) { f32x4 t = st[i]; sum += t[0] + t[2]; sq += t[1] + t[3]; }
    }
    const float mean = sum * (1.f / D_);
    const float var = fmaxf(sq * (1.f / D_) - mean * mean, 0.f);
    const float rstd = rsqrtf(var + LN_EPS);
    const int token = m0 + (wm * 4 + mt) * 16 + (lane & 15);
#pragma unroll
    for (int n = 0; n < 2; ++n) {
      float o[4];
#pragma unroll
      for (int r = 0; r < 4; ++r) {
        float g = rstd * (acc[0][n][mt][r] - mean * cg_[n][r]);
        float u = rstd * (acc[1][n][mt][r] - mean * cu_[n][r]);
        float y = g * (1.0f + 0.044715f * g * g);
        float e = __builtin_amdgcn_exp2f(y * (-2.0f * 0.7978845608028654f * 1.4426950408889634f));
        o[r] = g * __builtin_amdgcn_rcpf(1.0f + e) * u;
      }
      uint2 ov;
      ov.x = pack2(o[0], o[1]);
      ov.y = pack2(o[2], o[3]);
      *(uint2*)(aout + (size_t)token * DF_ + n0 + (wn * 2 + n) * 16 + quad * 4) = ov;
    }
  }
}

constexpr int AT_CHUNK = 512;
constexpr int AT_KA = 0, AT_KB = AT_CHUNK * 16, AT_KC = 2 * AT_CHUNK * 16;
constexpr int AT_VT = AT_KC + AT_CHUNK * 16;
constexpr int AT_VROW = AT_CHUNK * 2 + 16;
constexpr int AT_BUF = AT_VT + 21 * AT_VROW;
constexpr int AT_LDS = 2 * AT_BUF;
constexpr int AT_NSTEP = S_ / 64;
constexpr int AT_SPC = AT_CHUNK / 64;
constexpr float AT_THR = 8.0f;
constexpr int AT_NWAVE = 8;
constexpr int AT_TPW = 8 / AT_NWAVE;
constexpr int AT_GI = 48 / AT_NWAVE;

DI float other_half_max(float x) {
  auto r = __builtin_amdgcn_permlane32_swap(__float_as_uint(x), __float_as_uint(x), false, false);
  return fmaxf(__uint_as_float(r[0]), __uint_as_float(r[1]));
}
DI float max32(const f32x16 (&S)[2]) {
  float mx0 = fmaxf(S[0][0], S[0][1]), mx1 = fmaxf(S[0][8], S[0][9]);
  float mx2 = fmaxf(S[1][0], S[1][1]), mx3 = fmaxf(S[1][8], S[1][9]);
#pragma unroll
  for (int i = 2; i < 8; i += 2) {
    mx0 = fmaxf(fmaxf(mx0, S[0][i]), S[0][i + 1]);
    mx1 = fmaxf(fmaxf(mx1, S[0][8 + i]), S[0][8 + i + 1]);
    mx2 = fmaxf(fmaxf(mx2, S[1][i]), S[1][i + 1]);
    mx3 = fmaxf(fmaxf(mx3, S[1][8 + i]), S[1][8 + i + 1]);
  }
  return other_half_max(fmaxf(fmaxf(mx0, mx1), fmaxf(mx2, mx3)));
}

__global__ __launch_bounds__(AT_NWAVE * 64, 1) void k_attn(const char* __restrict__ ws_in, u16* __restrict__ oout) {
  extern __shared__ __attribute__((aligned(1024))) char smem[];
  const int tid = threadIdx.x, lane = tid & 63, wave = __builtin_amdgcn_readfirstlane(tid >> 6);
  const int h = lane >> 5, c = lane & 31;
  const int token0 = blockIdx.x * 8 + wave * AT_TPW;

  const char* src[AT_GI];
  int ldso[AT_GI], sstr[AT_GI];
#pragma unroll
  for (int j = 0; j < AT_GI; ++j) {
    int i = wave + j * AT_NWAVE;
    if (i >= 44) i -= AT_NWAVE;
    if (i < 8) {
      src[j] = ws_in + WS_KA + i * 1024 + lane * 16; ldso[j] = AT_KA + i * 1024; sstr[j] = AT_CHUNK * 16;
    } else if (i < 16) {
      src[j] = ws_in + WS_KB + (i - 8) * 1024 + lane * 16; ldso[j] = AT_KB + (i - 8) * 1024; sstr[j] = AT_CHUNK * 16;
    } else if (i < 24) {
      src[j] = ws_in + WS_KC + (i - 16) * 1024 + lane * 16; ldso[j] = AT_KC + (i - 16) * 1024; sstr[j] = AT_CHUNK * 16;
    } else {
      int d = i - 24;
      src[j] = ws_in + WS_VT + (size_t)d * S_ * 2 + lane * 16; ldso[j] = AT_VT + d * AT_VROW; sstr[j] = AT_CHUNK * 2;
    }
  }
  auto issue = [&](int buf) {
#pragma unroll
    for (int j = 0; j < AT_GI; ++j) {
      glds16(src[j], smem + buf * AT_BUF + ldso[j] + lane * 16);
      src[j] += sstr[j];
    }
  };
  issue(0);
  issue(1);

  if (tid < 128) {
    int b = tid >> 6;
    *(u32x4*)(smem + b * AT_BUF + AT_VT + 20 * AT_VROW + lane * 16) = u32x4{0x3f803f80u, 0x3f803f80u, 0x3f803f80u, 0x3f803f80u};
  }

  bf16x8 qf0[AT_TPW], qf1[AT_TPW];
#pragma unroll
  for (int u = 0; u < AT_TPW; ++u) {
    const u16* q = (const u16*)(ws_in + WS_Q) + (size_t)(token0 + u) * D_ + c * DH_;
    uint2 a = *(const uint2*)(q + 8 * h), b = *(const uint2*)(q + 8 * h + 4);
    qf0[u] = __builtin_bit_cast(bf16x8, u32x4{a.x, a.y, b.x, b.y});
    uint2 t = *(const uint2*)(q + 16);
    if (h) t = uint2{0u, 0u};
    qf1[u] = __builtin_bit_cast(bf16x8, u32x4{t.x, t.y, 0u, 0u});
  }

  const unsigned lb = lds_addr(smem);
  const unsigned kaddr = lb + (h ? AT_KB : AT_KA) + c * 16;
  const unsigned kcaddr = lb + AT_KC + c * 16;
  const unsigned vaddr = lb + AT_VT + (c < 20 ? c : 20) * AT_VROW + h * 16;

  f32x16 O[AT_TPW], Mb[AT_TPW];
#pragma unroll
  for (int u = 0; u < AT_TPW; ++u)
#pragma unroll
    for (int i = 0; i < 16; ++i) { O[u][i] = 0.f; Mb[u][i] = 0.f; }
  float m[AT_TPW];
#pragma unroll
  for (int u = 0; u < AT_TPW; ++u) m[u] = 0.f;

  asm volatile("s_waitcnt vmcnt(%0) lgkmcnt(0)" ::"n"(AT_GI) : "memory");
  RAW_BAR();

  bf16x8 k0[2], k1[2];
  f32x16 Sa[AT_TPW][2], Sb[AT_TPW][2];
  {
    lds_rd128<0>(k0[0], kaddr); lds_rd128<0>(k1[0], kcaddr);
    lds_rd128<512>(k0[1], kaddr); lds_rd128<512>(k1[1], kcaddr);
    asm volatile("s_waitcnt lgkmcnt(0)" ::: "memory");
    pin(k0[0]); pin(k1[0]); pin(k0[1]); pin(k1[1]);
#pragma unroll
    for (int u = 0; u < AT_TPW; ++u)
#pragma unroll
      for (int t = 0; t < 2; ++t) {
        Sa[u][t] = __builtin_amdgcn_mfma_f32_32x32x16_bf16(k0[t], qf0[u], Mb[u], 0, 0, 0);
        Sa[u][t] = __builtin_amdgcn_mfma_f32_32x32x16_bf16(k1[t], qf1[u], Sa[u][t], 0, 0, 0);
      }
  }

  auto step = [&](int n, f32x16 (&Sc)[AT_TPW][2], f32x16 (&Sn)[AT_TPW][2]) {
    const int ch = n / AT_SPC, st = n % AT_SPC;
    bf16x8 vf[2][2];
    {
      const unsigned va = vaddr + (ch & 1) * AT_BUF + st * 128;
      lds_rd128<0>(vf[0][0], va); lds_rd128<32>(vf[0][1], va);
      lds_rd128<64>(vf[1][0], va); lds_rd128<96>(vf[1][1], va);
    }
    const bool more = (n + 1 < AT_NSTEP);
    if (more) {
      if (st == AT_SPC - 1) {
        asm volatile("s_waitcnt vmcnt(0) lgkmcnt(0)" ::: "memory");
        RAW_BAR();
        if (ch + 2 < S_ / AT_CHUNK) issue(ch & 1);
      }
      const int n1 = n + 1;
      const unsigned b1 = ((n1 / AT_SPC) & 1) * AT_BUF + (n1 % AT_SPC) * 1024;
      lds_rd128<0>(k0[0], kaddr + b1); lds_rd128<0>(k1[0], kcaddr + b1);
      lds_rd128<512>(k0[1], kaddr + b1); lds_rd128<512>(k1[1], kcaddr + b1);
    }
    float mx[AT_TPW], mxa = 0.f;
#pragma unroll
    for (int u = 0; u < AT_TPW; ++u) { mx[u] = max32(Sc[u]); mxa = fmaxf(mxa, mx[u]); }
    if (n == 0 || __any(mxa > AT_THR)) {
#pragma unroll
      for (int u = 0; u < AT_TPW; ++u) {
        const float d = (n == 0) ? mx[u] : fmaxf(mx[u], 0.f);
        m[u] += d;
#pragma unroll
        for (int i = 0; i < 16; ++i) { Mb[u][i] = -m[u]; Sc[u][0][i] -= d; Sc[u][1][i] -= d; }
        if (n != 0) {
          const float f = __builtin_amdgcn_exp2f(-d);
#pragma unroll
          for (int i = 0; i < 16; ++i) O[u][i] *= f;
        }
      }
    }
    asm volatile("s_waitcnt lgkmcnt(0)" ::: "memory");
    pin(vf[0][0]); pin(vf[0][1]); pin(vf[1][0]); pin(vf[1][1]);
    if (more) {
      pin(k0[0]); pin(k1[0]); pin(k0[1]); pin(k1[1]);
#pragma unroll
      for (int u = 0; u < AT_TPW; ++u)
#pragma unroll
        for (int t = 0; t < 2; ++t) {
          Sn[u][t] = __builtin_amdgcn_mfma_f32_32x32x16_bf16(k0[t], qf0[u], Mb[u], 0, 0, 0);
          Sn[u][t] = __builtin_amdgcn_mfma_f32_32x32x16_bf16(k1[t], qf1[u], Sn[u][t], 0, 0, 0);
        }
    }
    __builtin_amdgcn_sched_barrier(0);
#pragma unroll
    for (int t = 0; t < 2; ++t)
#pragma unroll
      for (int s = 0; s < 2; ++s)
#pragma unroll
        for (int u = 0; u < AT_TPW; ++u) {
          u32x4 pk;
#pragma unroll
          for (int j = 0; j < 4; ++j)
            pk[j] = pack2(__builtin_amdgcn_exp2f(Sc[u][t][8 * s + 2 * j]), __builtin_amdgcn_exp2f(Sc[u][t][8 * s + 2 * j + 1]));
          O[u] = __builtin_amdgcn_mfma_f32_32x32x16_bf16(vf[t][s], __builtin_bit_cast(bf16x8, pk), O[u], 0, 0, 0);
        }
    __builtin_amdgcn_sched_barrier(0);
  };
  for (int n = 0; n < AT_NSTEP; n += 2) {
    step(n, Sa, Sb);
    step(n + 1, Sb, Sa);
  }

#pragma unroll
  for (int u = 0; u < AT_TPW; ++u) {
    auto rr = __builtin_amdgcn_permlane32_swap(__float_as_uint(O[u][8]), __float_as_uint(O[u][8]), false, false);
    const float inv = 1.0f / __uint_as_float(rr[1]);
    u16* o = oout + (size_t)(token0 + u) * D_ + c * DH_;
    uint2 v;
    v.x = pack2(O[u][0] * inv, O[u][1] * inv); v.y = pack2(O[u][2] * inv, O[u][3] * inv);
    *(uint2*)(o + 4 * h) = v;
    v.x = pack2(O[u][4] * inv, O[u][5] * inv); v.y = pack2(O[u][6] * inv, O[u][7] * inv);
    *(uint2*)(o + 8 + 4 * h) = v;
    if (h == 0) {
      v.x = pack2(O[u][8] * inv, O[u][9] * inv); v.y = pack2(O[u][10] * inv, O[u][11] * inv);
      *(uint2*)(o + 16) = v;
    }
  }
}

extern "C" void kernel_launch(void* const* d_in, const int* in_sizes, int n_in, void* d_out, int out_size,
                              void* d_ws, size_t ws_size, hipStream_t stream) {
  (void)in_sizes; (void)n_in; (void)out_size; (void)ws_size;
  const u16* x0 = (const u16*)d_in[0];
  const u16* wq = (const u16*)d_in[1];
  const u16* wk = (const u16*)d_in[2];
  const u16* wv = (const u16*)d_in[3];
  const u16* wo = (const u16*)d_in[4];
  const u16* wg = (const u16*)d_in[6];
  const u16* wu = (const u16*)d_in[7];
  const u16* wd = (const u16*)d_in[8];
  char* ws = (char*)d_ws;
  u16* out = (u16*)d_out;

  static bool attr_done = false;
  if (!attr_done) {
    hipFuncSetAttribute((const void*)k_qkv, hipFuncAttributeMaxDynamicSharedMemorySize, CfgS::LDS_BYTES);
    hipFuncSetAttribute((const void*)k_proj_res<D_>, hipFuncAttributeMaxDynamicSharedMemorySize, CfgS::LDS_BYTES);
    hipFuncSetAttribute((const void*)k_proj_res<DF_>, hipFuncAttributeMaxDynamicSharedMemorySize, CfgS::LDS_BYTES);
    hipFuncSetAttribute((const void*)k_gateup, hipFuncAttributeMaxDynamicSharedMemorySize, CfgF::LDS_BYTES);
    hipFuncSetAttribute((const void*)k_attn, hipFuncAttributeMaxDynamicSharedMemorySize, AT_LDS);
    attr_done = true;
  }

  const u16* xin = x0;
  for (int layer = 0; layer < 2; ++layer) {
    u16* x1 = (u16*)(ws + WS_X1);
    u16* x2 = (layer == 1) ? out : (u16*)(ws + WS_X2);
    k_qkv<<<layer == 0 ? 288 + 54 : 288, CfgS::THREADS, CfgS::LDS_BYTES, stream>>>(xin, wq, wk, wv, wg, wu, ws);
    k_attn<<<256, AT_NWAVE * 64, AT_LDS, stream>>>(ws, (u16*)(ws + WS_O));
    k_proj_res<D_><<<256, CfgS::THREADS, CfgS::LDS_BYTES, stream>>>((const u16*)(ws + WS_O), wo, xin, x1, (float2*)(ws + WS_ST));
    k_gateup<<<216, CfgF::THREADS, CfgF::LDS_BYTES, stream>>>(x1, wg, wu, (u16*)(ws + WS_A), ws);
    k_proj_res<DF_><<<256, CfgS::THREADS, CfgS::LDS_BYTES, stream>>>((const u16*)(ws + WS_A), wd, x1, x2, (float2*)nullptr);
    xin = x2;
  }
}
```
